```python
import torch

def _softmax(x, axis=-1):
    return torch.softmax(x, dim=axis)
def _gelu(x):
    return torch.nn.functional.gelu(x, approximate="tanh")
def _silu(x):
    return torch.nn.functional.silu(x)
def _sigmoid(x):
    return torch.sigmoid(x)
def _layernorm(x, axis=-1, eps=1e-5):
    m = x.mean(dim=axis, keepdim=True)
    v = x.var(dim=axis, keepdim=True, unbiased=False)
    return (x - m) / torch.sqrt(v + eps)
def _scatter_add(tgt, idx, src, axis=0):
    return tgt.clone().index_add_(axis, idx, src)
def _segment_sum(src, seg, n):
    out = torch.zeros((n,) + src.shape[1:], dtype=src.dtype)
    return out.index_add_(0, seg, src)
def _pad(x, pw):
    flat = tuple(v for pair in reversed(pw) for v in pair)
    return torch.nn.functional.pad(x, flat)

kernel_name = 'recurrent_scan__sliding_block_cached__mixer_token_mlp__decode'

def setup_inputs():

    _w = (lambda a: (lambda t: t.to(torch.bfloat16) if t.is_floating_point() else t)(torch.as_tensor(a)))
    def _A_setup():
        B, Sq, D = 1, 1, 640
        Hq, Hkv, Sk, Dh = 32, 32, 32768, 192
        assert Hq % Hkv == 0, (Hq, Hkv)
        return {
            "x": _w((lambda *s: torch.randn(*s, dtype=torch.bfloat16) * 0.1)(B, Sq, D)),
            "k_cache": _w((lambda *s: torch.randn(*s, dtype=torch.bfloat16) * 0.8)(B, Hkv, Sk, Dh)),
            "v_cache": _w((lambda *s: torch.randn(*s, dtype=torch.bfloat16) * 3.2)(B, Hkv, Sk, Dh)),
            "wq": _w((lambda *s: torch.randn(*s, dtype=torch.bfloat16) * 0.8)(D, Hq * Dh) * (D ** -0.5)),
            "wo": _w((lambda *s: torch.randn(*s, dtype=torch.bfloat16) * 3.2)(Hq * Dh, D) * ((Hq * Dh) ** -0.5)),
        }
    _A_in = _A_setup()

    def _B_setup():
        B, S, D, F = 1, 1, 640, 1536
        return {
            "x": _w((lambda *s: torch.randn(*s, dtype=torch.bfloat16) * 0.1)(B, S, D)),
            "w1": _w((lambda *s: torch.randn(*s, dtype=torch.bfloat16) * 3.2)(S, F) * (S ** -0.5)),
            "b1": _w((lambda *s: torch.randn(*s, dtype=torch.bfloat16) * 3.2)(F) * 0.02),
            "w2": _w((lambda *s: torch.randn(*s, dtype=torch.bfloat16) * 3.2)(F, S) * (F ** -0.5)),
            "b2": _w((lambda *s: torch.randn(*s, dtype=torch.bfloat16) * 3.2)(S) * 0.02),
        }
    _B_in = _B_setup()

    return {
        "A_x": _A_in["x"],
        "A_k_cache": _A_in["k_cache"],
        "A_v_cache": _A_in["v_cache"],
        "A_wq": _A_in["wq"],
        "A_wo": _A_in["wo"],
        "B_x": _B_in["x"],
        "B_w1": _B_in["w1"],
        "B_b1": _B_in["b1"],
        "B_w2": _B_in["w2"],
        "B_b2": _B_in["b2"],
    }

def reference(A_x, A_k_cache, A_v_cache, A_wq, A_wo, B_x, B_w1, B_b1, B_w2, B_b2):
    def _A_ref(x, k_cache, v_cache, wq, wo):
        B, Sq, D = x.shape
        Hq, Dh = 32, 192
        Hkv, Sk = k_cache.shape[1], k_cache.shape[2]
        W = min(4096, max(256, Sk // 4))
        rep = Hq // Hkv
        q = (lambda x, *d: x.permute(*d))((x @ wq).reshape(B, Sq, Hq, Dh), 0, 2, 1, 3)
        k = (lambda x, n, axis: torch.repeat_interleave(x, n, dim=axis))(k_cache[:, :, Sk - W:, :], rep, axis=1)
        v = (lambda x, n, axis: torch.repeat_interleave(x, n, dim=axis))(v_cache[:, :, Sk - W:, :], rep, axis=1)
        scores = torch.einsum("bhqd,bhkd->bhqk", q, k) * (Dh ** -0.5)
        p = _softmax(scores, axis=-1)
        o = torch.einsum("bhqk,bhkd->bhqd", p, v)
        return (lambda x, *d: x.permute(*d))(o, 0, 2, 1, 3).reshape(B, Sq, Hq * Dh) @ wo

    def _B_ref(x, w1, b1, w2, b2):
        B, S, D = x.shape
        h = _layernorm(x, axis=-1)
        u = torch.einsum("bsd,sf->bdf", h, w1) + b1
        u = _gelu(u)
        y = torch.einsum("bdf,fs->bds", u, w2) + b2
        return (lambda x, *d: x.permute(*d))(y, 0, 2, 1)

    x = A_x
    xn = _layernorm(x, axis=-1)
    s = _A_ref(xn, A_k_cache, A_v_cache, A_wq, A_wo)
    g = _silu(_B_ref(xn, B_w1, B_b1, B_w2, B_b2))
    return x + s * g
```

```cpp
#include <hip/hip_runtime.h>
#include <hip/hip_bf16.h>
#include <stdint.h>

typedef unsigned int u32;
typedef unsigned long long u64;
typedef __attribute__((ext_vector_type(2))) u32 u32x2;
typedef __attribute__((ext_vector_type(4))) u32 u32x4;
typedef __attribute__((ext_vector_type(4))) float f32x4;
typedef __attribute__((ext_vector_type(2))) float f32x2;
typedef __attribute__((ext_vector_type(2))) __bf16 bf16x2;

constexpr int D_MODEL = 640, NH = 32, DH = 192, SK = 32768, WIN = 4096;
constexpr int NSPLIT = 8, ROWS_WG = WIN / NSPLIT;
constexpr int ROW_B = DH * 2;
constexpr int WQ_ROW_B = NH * DH * 2;
constexpr int KV_AUX = 2;
constexpr int SC1 = 16;
constexpr int K0 = 8;
constexpr int KE = 16;
constexpr int VE = 8;
constexpr int VD = 20;
constexpr int WOD = 0;
static_assert(VD >= VE && VD - VE <= 12 && VD <= 24, "V issue schedule");
constexpr u32 WS_EPOCH = 0;
constexpr u32 WS_HOP1 = 4096;
constexpr u32 HOP1_STRIDE = 2048;
constexpr u32 WS_HOP2 = WS_HOP1 + NH * NSPLIT * HOP1_STRIDE;
constexpr u32 HOP2_STRIDE = 640;
constexpr u32 WS_HOP0 = WS_HOP2 + NH * NSPLIT * HOP2_STRIDE;
constexpr u32 HOP0_STRIDE = 1536;
constexpr u32 WS_END = WS_HOP0 + NH * NSPLIT * HOP0_STRIDE;

constexpr int L_XS = 0;
constexpr int L_HS = L_XS + 640;
constexpr int L_YP = L_HS + 32;
constexpr int L_WP = L_YP + 32;
constexpr int L_QS = L_WP + 1536;
constexpr int L_MM = L_QS + 96;
constexpr int L_ML = L_MM + 8;
constexpr int L_GS = L_ML + 8;
constexpr int L_PT = L_GS + 1536;
constexpr int L_OS = L_PT + 512;
constexpr int L_RED = L_OS + 192;
constexpr int L_TAG = L_RED + 2560;
constexpr int L_MA = L_TAG + 4;
constexpr int L_TOTAL = L_MA + 8 * 1536;

__device__ __forceinline__ float bf16r(float v) { return (float)(__bf16)v; }
__device__ __forceinline__ float lo16(u32 w) { return __uint_as_float(w << 16); }
__device__ __forceinline__ float hi16(u32 w) { return __uint_as_float(w & 0xffff0000u); }
__device__ __forceinline__ float dot2(u32 a, u32 b, float c) {
  return __builtin_amdgcn_fdot2_f32_bf16(__builtin_bit_cast(bf16x2, a), __builtin_bit_cast(bf16x2, b), c, false);
}
template <int CTRL> __device__ __forceinline__ float dpp(float x) {
  return __builtin_bit_cast(float, __builtin_amdgcn_mov_dpp(__builtin_bit_cast(int, x), CTRL, 0xf, 0xf, true));
}
__device__ __forceinline__ float swap16_sum(float x) {
  auto s = __builtin_amdgcn_permlane16_swap(__float_as_uint(x), __float_as_uint(x), false, false);
  return __uint_as_float(s[0]) + __uint_as_float(s[1]);
}
__device__ __forceinline__ float swap32_sum(float x) {
  auto s = __builtin_amdgcn_permlane32_swap(__float_as_uint(x), __float_as_uint(x), false, false);
  return __uint_as_float(s[0]) + __uint_as_float(s[1]);
}
__device__ __forceinline__ float swap16_max(float x) {
  auto s = __builtin_amdgcn_permlane16_swap(__float_as_uint(x), __float_as_uint(x), false, false);
  return fmaxf(__uint_as_float(s[0]), __uint_as_float(s[1]));
}
__device__ __forceinline__ float swap32_max(float x) {
  auto s = __builtin_amdgcn_permlane32_swap(__float_as_uint(x), __float_as_uint(x), false, false);
  return fmaxf(__uint_as_float(s[0]), __uint_as_float(s[1]));
}
__device__ __forceinline__ float ar8_sum(float x) {
  x += dpp<0xB1>(x); x += dpp<0x4E>(x); x += dpp<0x141>(x);
  return x;
}
__device__ __forceinline__ float ar64_sum(float x) {
  x = ar8_sum(x); x += dpp<0x140>(x);
  return swap32_sum(swap16_sum(x));
}
__device__ __forceinline__ float ar64_max(float x) {
  x = fmaxf(x, dpp<0xB1>(x)); x = fmaxf(x, dpp<0x4E>(x)); x = fmaxf(x, dpp<0x141>(x)); x = fmaxf(x, dpp<0x140>(x));
  return swap32_max(swap16_max(x));
}
__device__ __forceinline__ u32 opaque0() { u32 z = 0; asm volatile("" : "+s"(z) :: "memory"); return z; }
__device__ __forceinline__ void lds_fence() { asm volatile("s_waitcnt lgkmcnt(0)" ::: "memory"); }
__device__ __forceinline__ void wg_barrier() {
  asm volatile("s_waitcnt lgkmcnt(0)" ::: "memory");
  __builtin_amdgcn_s_barrier();
  asm volatile("" ::: "memory");
}
__device__ __forceinline__ void fma8(float (&a)[8], float s, const u32x4& w) {
  a[0] = fmaf(s, lo16(w.x), a[0]); a[1] = fmaf(s, hi16(w.x), a[1]);
  a[2] = fmaf(s, lo16(w.y), a[2]); a[3] = fmaf(s, hi16(w.y), a[3]);
  a[4] = fmaf(s, lo16(w.z), a[4]); a[5] = fmaf(s, hi16(w.z), a[5]);
  a[6] = fmaf(s, lo16(w.w), a[6]); a[7] = fmaf(s, hi16(w.w), a[7]);
}

#define PIN24(a) asm volatile("" : "+v"(a[0][0]), "+v"(a[0][1]), "+v"(a[0][2]), "+v"(a[0][3]), "+v"(a[0][4]), "+v"(a[0][5]), \
    "+v"(a[0][6]), "+v"(a[0][7]), "+v"(a[1][0]), "+v"(a[1][1]), "+v"(a[1][2]), "+v"(a[1][3]), "+v"(a[1][4]), "+v"(a[1][5]), \
    "+v"(a[1][6]), "+v"(a[1][7]), "+v"(a[2][0]), "+v"(a[2][1]), "+v"(a[2][2]), "+v"(a[2][3]), "+v"(a[2][4]), "+v"(a[2][5]), \
    "+v"(a[2][6]), "+v"(a[2][7]) :: "memory")

__device__ __forceinline__ void ln10(const float (&v)[10], float (&o)[10]) {
  float s = 0.f;
#pragma unroll
  for (int i = 0; i < 10; ++i) s += v[i];
  const float mean = ar64_sum(s) * (1.0f / 640.0f);
  float q = 0.f;
#pragma unroll
  for (int i = 0; i < 10; ++i) { const float d = v[i] - mean; q += d * d; }
  const float var = ar64_sum(q) * (1.0f / 640.0f);
  const float mb = bf16r(mean), vb = bf16r(var);
  const float den = bf16r(sqrtf(bf16r(vb + 1e-5f)));
#pragma unroll
  for (int i = 0; i < 10; ++i) o[i] = bf16r(bf16r(v[i] - mb) / den);
}

__device__ __forceinline__ void wave_piece_reduce(float* lds, int wave, int lane, const float (&a)[3][8]) {
  float* ma = lds + L_MA + wave * 1536;
#pragma unroll
  for (int j = 0; j < 3; ++j) {
    *(f32x4*)&ma[(64 * j + lane) * 8] = f32x4{a[j][0], a[j][1], a[j][2], a[j][3]};
    *(f32x4*)&ma[(64 * j + lane) * 8 + 4] = f32x4{a[j][4], a[j][5], a[j][6], a[j][7]};
  }
  lds_fence();
  if (lane < 48) {
    f32x4 s = *(const f32x4*)&ma[lane * 4];
#pragma unroll
    for (int r = 1; r < 8; ++r) s += *(const f32x4*)&ma[r * 192 + lane * 4];
    *(f32x4*)&lds[L_WP + wave * 192 + lane * 4] = s;
  }
}

__global__ void __launch_bounds__(512, 2)
fused_decode_kernel(const u32* __restrict__ x, const u32* __restrict__ kc, const u32* __restrict__ vc,
                    const u32* __restrict__ wq, const u32* __restrict__ wo, const u32* __restrict__ w1,
                    const u32* __restrict__ b1, const u32* __restrict__ w2, const unsigned short* __restrict__ b2,
                    u32* __restrict__ out, unsigned char* __restrict__ ws) {
  __shared__ __attribute__((aligned(16))) float lds[L_TOTAL];
  const int bid = blockIdx.x;
  const int h = (bid & 7) * 4 + ((bid >> 3) & 3);
  const int c = bid >> 5;
  const int tid = threadIdx.x, lane = tid & 63;
  const int wave = __builtin_amdgcn_readfirstlane(tid >> 6);
  const bool reducer = h < 20;

  const size_t kv_off = ((size_t)h * SK + (SK - WIN) + (size_t)c * ROWS_WG) * (DH / 2);
  auto rk = __builtin_amdgcn_make_buffer_rsrc((void*)(kc + kv_off), 0, ROWS_WG * ROW_B, 0x00020000);
  auto rv = __builtin_amdgcn_make_buffer_rsrc((void*)(vc + kv_off), 0, ROWS_WG * ROW_B, 0x00020000);
  auto rq = __builtin_amdgcn_make_buffer_rsrc((void*)(wq + h * (DH / 2)), 0, (D_MODEL * NH * DH - h * DH) * 2, 0x00020000);
  auto ro = __builtin_amdgcn_make_buffer_rsrc((void*)(wo + (size_t)h * DH * (D_MODEL / 2) + c * 40), 0,
                                              (DH * D_MODEL - c * 80) * 2, 0x00020000);
  auto rw = __builtin_amdgcn_make_buffer_rsrc((void*)ws, 0, WS_END, 0x00020000);

  int rs[3], cp[3];
#pragma unroll
  for (int j = 0; j < 3; ++j) { rs[j] = (64 * j + lane) / 24; cp[j] = (64 * j + lane) % 24; }

  const u32 kvo = wave * (64 * ROW_B) + lane * 16;
  u32x4 K[24], V[24], WQ[4], WO[6];
#pragma unroll
  for (int n = 0; n < K0; ++n) K[n] = __builtin_amdgcn_raw_buffer_load_b128(rk, kvo, n * 1024, KV_AUX);
  __builtin_amdgcn_sched_barrier(0);
  u32 xr[5];
#pragma unroll
  for (int i = 0; i < 5; ++i) xr[i] = x[lane * 5 + i];
  u32x2 w1r = {0, 0}, b1r = {0, 0}, w2r = {0, 0};
  if (reducer && tid < 384) {
    w1r = *(const u32x2*)(w1 + tid * 2);
    b1r = *(const u32x2*)(b1 + tid * 2);
    w2r = *(const u32x2*)(w2 + tid * 2);
  }
  const u32x2 ep = __builtin_amdgcn_raw_buffer_load_b64(rw, WS_EPOCH, 0, SC1);
  const u32 b2r = b2[0];
  const u32 xw = x[reducer ? (c * 80 + 4 * h) / 2 + (lane >> 5) : 0];
  __builtin_amdgcn_sched_barrier(0);

  {
    const u32 rb = (u32)(c * 80 + wave * 10) * WQ_ROW_B;
#pragma unroll
    for (int j = 0; j < 3; ++j)
      WQ[j] = __builtin_amdgcn_raw_buffer_load_b128(rq, rb + rs[j] * WQ_ROW_B + cp[j] * 16, 0, KV_AUX);
    WQ[3] = __builtin_amdgcn_raw_buffer_load_b128(
        rq, lane < 48 ? rb + (8 + rs[0]) * WQ_ROW_B + cp[0] * 16 : 0x7fff0000u, 0, KV_AUX);
  }
  __builtin_amdgcn_sched_barrier(0);
#pragma unroll
  for (int n = K0; n < KE; ++n) K[n] = __builtin_amdgcn_raw_buffer_load_b128(rk, kvo, n * 1024, KV_AUX);
  __builtin_amdgcn_sched_barrier(0);
  {
    float xv[10], xn[10];
#pragma unroll
    for (int i = 0; i < 5; ++i) { xv[2 * i] = lo16(xr[i]); xv[2 * i + 1] = hi16(xr[i]); }
    ln10(xv, xn);
    if (lane == c * 8 + wave) {
#pragma unroll
      for (int i = 0; i < 10; ++i) lds[L_XS + wave * 16 + i] = xn[i];
    }
    if (tid == 0) {
      const u64 tg = (((u64)ep.y << 32) | ep.x) + 1;
      ((u32*)lds)[L_TAG] = (u32)tg; ((u32*)lds)[L_TAG + 1] = (u32)(tg >> 32);
    }
    if (reducer) {
      float s2 = 0.f;
#pragma unroll
      for (int i = 0; i < 10; ++i) s2 += xn[i];
      const float mean = ar64_sum(s2) * (1.0f / 640.0f);
      float q2 = 0.f;
#pragma unroll
      for (int i = 0; i < 10; ++i) { const float d = xn[i] - mean; q2 += d * d; }
      const float var = ar64_sum(q2) * (1.0f / 640.0f);
      const float mb = bf16r(mean), den = bf16r(sqrtf(bf16r(bf16r(var) + 1e-5f)));
      const int col0 = c * 80 + 4 * h;
#pragma unroll
      for (int i = 0; i < 10; ++i) {
        const int e = lane * 10 + i - col0;
        if (e >= 0 && e < 4) lds[L_HS + wave * 4 + e] = bf16r(bf16r(xn[i] - mb) / den);
      }
    }
    lds_fence();
    {
      float qa[3][8];
#pragma unroll
      for (int j = 0; j < 3; ++j)
#pragma unroll
        for (int e = 0; e < 8; ++e) qa[j][e] = 0.f;
#pragma unroll
      for (int j = 0; j < 3; ++j) fma8(qa[j], lds[L_XS + wave * 16 + rs[j]], WQ[j]);
      fma8(qa[0], lds[L_XS + wave * 16 + 8 + (rs[0] & 1)], WQ[3]);
      wave_piece_reduce(lds, wave, lane, qa);
    }
    wg_barrier();
    const u32 tag_lo = ((const u32*)lds)[L_TAG], tag_hi = ((const u32*)lds)[L_TAG + 1];
    if (wave == 0) {
      const u32 base0 = WS_HOP0 + (u32)(h * NSPLIT) * HOP0_STRIDE;
      const int t1 = lane < 32 ? 64 + lane : lane;
      float a0 = 0.f, a1 = 0.f, b0 = 0.f, b1v = 0.f;
#pragma unroll
      for (int w = 0; w < 8; ++w) {
        const f32x2 t = *(const f32x2*)&lds[L_WP + w * 192 + 2 * lane];
        const f32x2 u = *(const f32x2*)&lds[L_WP + w * 192 + 2 * t1];
        a0 += t.x; a1 += t.y; b0 += u.x; b1v += u.y;
      }
      u32x4 g;
      g.x = __float_as_uint(a0); g.y = __float_as_uint(a1); g.z = tag_lo; g.w = tag_hi;
      __builtin_amdgcn_raw_buffer_store_b128(g, rw, base0 + c * HOP0_STRIDE + lane * 16, 0, SC1);
      if (lane < 32) {
        g.x = __float_as_uint(b0); g.y = __float_as_uint(b1v);
        __builtin_amdgcn_raw_buffer_store_b128(g, rw, base0 + c * HOP0_STRIDE + t1 * 16, 0, SC1);
      }
    }
    asm volatile("" ::: "memory");
#pragma unroll
    for (int n = KE; n < 24; ++n) K[n] = __builtin_amdgcn_raw_buffer_load_b128(rk, kvo, n * 1024, KV_AUX);
    asm volatile("" ::: "memory");
    if (reducer) {
      const f32x4 hs = *(const f32x4*)&lds[L_HS + wave * 4];
      const float w1f[4] = {lo16(w1r.x), hi16(w1r.x), lo16(w1r.y), hi16(w1r.y)};
      const float b1f[4] = {lo16(b1r.x), hi16(b1r.x), lo16(b1r.y), hi16(b1r.y)};
      const float w2f[4] = {lo16(w2r.x), hi16(w2r.x), lo16(w2r.y), hi16(w2r.y)};
      float ys[4];
#pragma unroll
      for (int j = 0; j < 4; ++j) {
        float a = 0.f;
#pragma unroll
        for (int f = 0; f < 4; ++f) {
          const float u = bf16r(bf16r(hs[j] * w1f[f]) + b1f[f]);
          const float z2 = u * fmaf(u * u, 2.0f * 0.7978845608028654f * 0.044715f * 1.4426950408889634f,
                                    2.0f * 0.7978845608028654f * 1.4426950408889634f);
          const float r = __builtin_amdgcn_rcpf(1.0f + __builtin_amdgcn_exp2f(z2));
          const float ge = bf16r(fmaf(-u, r, u));
          a = fmaf(ge, w2f[f], a);
        }
        ys[j] = ar64_sum(a);
      }
      if (lane == 0) *(f32x4*)&lds[L_YP + wave * 4] = f32x4{ys[0], ys[1], ys[2], ys[3]};
    }
    asm volatile("" ::: "memory");
#define SCORES do { \
      u32x4 qf[3]; \
      _Pragma("unroll") for (int j = 0; j < 3; ++j) qf[j] = *(const u32x4*)&((const u32*)lds)[L_QS + cp[j] * 4]; \
      _Pragma("unroll") for (int n = 0; n < 24; ++n) { \
        const int j = n % 3; \
        float d = dot2(K[n].x, qf[j].x, 0.f); d = dot2(K[n].y, qf[j].y, d); \
        d = dot2(K[n].z, qf[j].z, d); d = dot2(K[n].w, qf[j].w, d); \
        d = ar8_sum(d); \
        if ((lane & 7) == 0) lds[L_GS + wave * 192 + n * 8 + (lane >> 3)] = d; \
        if (n % 2 == 1 && VE + n / 2 < VD) { \
          asm volatile("" ::: "memory"); \
          V[VE + n / 2] = __builtin_amdgcn_raw_buffer_load_b128(rv, kvo, (VE + n / 2) * 1024, KV_AUX); \
        } \
      } \
    } while (0)
    if (wave == 0) {
      const u32 base0 = WS_HOP0 + (u32)(h * NSPLIT) * HOP0_STRIDE;
      const int t1 = lane < 32 ? 64 + lane : lane;
      u32x4 ga[8], gb[8];
      for (u32 spins = 0; spins < (1u << 16); ++spins) {
        const u32 z = opaque0();
        bool ok = true;
#pragma unroll
        for (int s = 0; s < 8; ++s) {
          ga[s] = __builtin_amdgcn_raw_buffer_load_b128(rw, base0 + s * HOP0_STRIDE + lane * 16, z, SC1);
          gb[s] = __builtin_amdgcn_raw_buffer_load_b128(rw, base0 + s * HOP0_STRIDE + t1 * 16, z, SC1);
        }
#pragma unroll
        for (int s = 0; s < 8; ++s)
          ok = ok && ga[s].z == tag_lo && ga[s].w == tag_hi && gb[s].z == tag_lo && gb[s].w == tag_hi;
        if (__all(ok)) break;
        __builtin_amdgcn_s_sleep(1);
      }
      float a0 = 0.f, a1 = 0.f, b0 = 0.f, b1v = 0.f;
#pragma unroll
      for (int s = 0; s < 8; ++s) {
        a0 += __uint_as_float(ga[s].x); a1 += __uint_as_float(ga[s].y);
        b0 += __uint_as_float(gb[s].x); b1v += __uint_as_float(gb[s].y);
      }
      ((u32*)lds)[L_QS + lane] = (__float_as_uint(bf16r(a0)) >> 16) | (__float_as_uint(bf16r(a1)) & 0xffff0000u);
      if (lane < 32)
        ((u32*)lds)[L_QS + t1] = (__float_as_uint(bf16r(b0)) >> 16) | (__float_as_uint(bf16r(b1v)) & 0xffff0000u);
    }
    asm volatile("" ::: "memory");
    u32x4 V1[VE];
#pragma unroll
    for (int n = 0; n < VE; ++n)
      V1[n] = __builtin_amdgcn_raw_buffer_load_b128(rv, wave != 0 ? kvo : 0x7fff0000u, n * 1024, KV_AUX);
    asm volatile("" ::: "memory");
    wg_barrier();
#pragma unroll
    for (int n = 0; n < VE; ++n)
      V[n] = __builtin_amdgcn_raw_buffer_load_b128(rv, wave == 0 ? kvo : 0x7fff0000u, n * 1024, KV_AUX);
    SCORES;
#pragma unroll
    for (int n = 0; n < VE; ++n) V[n] |= V1[n];
  }
  const u32 tag_lo = ((const u32*)lds)[L_TAG], tag_hi = ((const u32*)lds)[L_TAG + 1];
  __builtin_amdgcn_sched_barrier(0);
  lds_fence();
  const float LOG2E = 1.4426950408889634f;
  float m, l;
  {
    const float* gs = &lds[L_GS + wave * 192 + 3 * lane];
    float s = gs[0] + gs[1] + gs[2];
    s = bf16r(bf16r(s) * 0.07216878364870322f);
    m = ar64_max(s);
    const float p = __builtin_amdgcn_exp2f((s - m) * LOG2E);
    l = ar64_sum(p);
    lds[L_PT + wave * 64 + (lane & 7) * 8 + (lane >> 3)] = p;
  }
  lds_fence();
  const u32 woo = (lane & 15) < 10 ? (u32)((wave * 24 + (lane >> 4)) * (D_MODEL * 2) + (lane & 15) * 16) : 0x7fff0000u;
  {
    float P[3][8];
#pragma unroll
    for (int j = 0; j < 3; ++j) {
      const f32x4 a = *(const f32x4*)&lds[L_PT + wave * 64 + rs[j] * 8];
      const f32x4 b = *(const f32x4*)&lds[L_PT + wave * 64 + rs[j] * 8 + 4];
      P[j][0] = a.x; P[j][1] = a.y; P[j][2] = a.z; P[j][3] = a.w;
      P[j][4] = b.x; P[j][5] = b.y; P[j][6] = b.z; P[j][7] = b.w;
    }
    float acc[3][8];
#pragma unroll
    for (int j = 0; j < 3; ++j)
#pragma unroll
      for (int e = 0; e < 8; ++e) acc[j][e] = 0.f;
#pragma unroll
    for (int n = 0; n < 24; ++n) {
      fma8(acc[n % 3], P[n % 3][n / 3], V[n]);
      if (n + VD < 24) {
        asm volatile("" : "+v"(acc[n % 3][0]), "+v"(acc[n % 3][7]) :: "memory");
        V[n + VD] = __builtin_amdgcn_raw_buffer_load_b128(rv, kvo, (n + VD) * 1024, KV_AUX);
      }
      if (n + VD == 23 + WOD) {
        asm volatile("" : "+v"(acc[n % 3][0]), "+v"(acc[n % 3][7]) :: "memory");
#pragma unroll
        for (int k = 0; k < 6; ++k) WO[k] = __builtin_amdgcn_raw_buffer_load_b128(ro, woo, k * 4 * (D_MODEL * 2), KV_AUX);
      }
    }
    PIN24(acc);
    wave_piece_reduce(lds, wave, lane, acc);
    if (lane == 0) { lds[L_MM + wave] = m; lds[L_ML + wave] = l; }
  }
  wg_barrier();
  if (tid < 128) {
    float M = -INFINITY;
#pragma unroll
    for (int i = 0; i < 8; ++i) M = fmaxf(M, lds[L_MM + i]);
    float L = 0.f, a0 = 0.f, a1 = 0.f;
    const int t = tid < 96 ? tid : 0;
#pragma unroll
    for (int i = 0; i < 8; ++i) {
      const float w = __builtin_amdgcn_exp2f((lds[L_MM + i] - M) * LOG2E);
      const f32x2 a = *(const f32x2*)&lds[L_WP + i * 192 + 2 * t];
      L = fmaf(w, lds[L_ML + i], L);
      a0 = fmaf(w, a.x, a0);
      a1 = fmaf(w, a.y, a1);
    }
    const u32 base1 = WS_HOP1 + (u32)(h * NSPLIT) * HOP1_STRIDE;
    if (tid <= 96) {
      u32x4 g;
      g.x = __float_as_uint(tid < 96 ? a0 : M); g.y = __float_as_uint(tid < 96 ? a1 : L); g.z = tag_lo; g.w = tag_hi;
      __builtin_amdgcn_raw_buffer_store_b128(g, rw, base1 + c * HOP1_STRIDE + tid * 16, 0, SC1);
    }
  }
  f32x2 wf[6][4];
#pragma unroll
  for (int k = 0; k < 6; ++k) {
    wf[k][0] = f32x2{lo16(WO[k].x), hi16(WO[k].x)}; wf[k][1] = f32x2{lo16(WO[k].y), hi16(WO[k].y)};
    wf[k][2] = f32x2{lo16(WO[k].z), hi16(WO[k].z)}; wf[k][3] = f32x2{lo16(WO[k].w), hi16(WO[k].w)};
#pragma unroll
    for (int e = 0; e < 4; ++e) asm volatile("" : "+v"(wf[k][e]));
  }
  float g0 = 0.f, g1 = 0.f;
  if (reducer && wave == 0) {
    const int pr = lane >> 5;
    float y0 = 0.f, y1 = 0.f;
#pragma unroll
    for (int w = 0; w < 6; ++w) { y0 += lds[L_YP + w * 4 + 2 * pr]; y1 += lds[L_YP + w * 4 + 2 * pr + 1]; }
    const float b2f = lo16(b2r);
    y0 = bf16r(bf16r(y0) + b2f); y1 = bf16r(bf16r(y1) + b2f);
    g0 = bf16r(y0 / (1.0f + __expf(-y0))); g1 = bf16r(y1 / (1.0f + __expf(-y1)));
    asm volatile("" : "+v"(g0), "+v"(g1));
  }
  {
    const u32 base1 = WS_HOP1 + (u32)(h * NSPLIT + wave) * HOP1_STRIDE;
    const u32 o1 = lane < 33 ? base1 + (64 + lane) * 16 : 0x7fff0000u;
    u32x4 g0, g1;
    for (u32 spins = 0; spins < (1u << 16); ++spins) {
      const u32 z = opaque0();
      g0 = __builtin_amdgcn_raw_buffer_load_b128(rw, base1 + lane * 16, z, SC1);
      g1 = __builtin_amdgcn_raw_buffer_load_b128(rw, o1, z, SC1);
      const bool ok = g0.z == tag_lo && g0.w == tag_hi && (lane >= 33 || (g1.z == tag_lo && g1.w == tag_hi));
      if (__all(ok)) break;
      __builtin_amdgcn_s_sleep(1);
    }
    float* ha = lds + L_MA + wave * 200;
    *(f32x2*)&ha[2 * lane] = f32x2{__uint_as_float(g0.x), __uint_as_float(g0.y)};
    if (lane < 33) *(f32x2*)&ha[128 + 2 * lane] = f32x2{__uint_as_float(g1.x), __uint_as_float(g1.y)};
  }
  wg_barrier();
  {
    float MM = -INFINITY;
#pragma unroll
    for (int s = 0; s < 8; ++s) MM = fmaxf(MM, lds[L_MA + s * 200 + 192]);
    float LL = 0.f, o0 = 0.f;
    const int row = wave * 24 + (lane < 24 ? lane : 0);
#pragma unroll
    for (int s = 0; s < 8; ++s) {
      const float w = __builtin_amdgcn_exp2f((lds[L_MA + s * 200 + 192] - MM) * LOG2E);
      LL = fmaf(w, lds[L_MA + s * 200 + 193], LL);
      o0 = fmaf(w, lds[L_MA + s * 200 + row], o0);
    }
    if (lane < 24) lds[L_OS + row] = bf16r(o0 * (1.0f / LL));
    lds_fence();
  }
  {
    const int r4 = lane >> 4;
    f32x2 p[4] = {{0.f, 0.f}, {0.f, 0.f}, {0.f, 0.f}, {0.f, 0.f}};
#pragma unroll
    for (int k = 0; k < 6; ++k) {
      const float ov = lds[L_OS + wave * 24 + k * 4 + r4];
      const f32x2 o2 = {ov, ov};
#pragma unroll
      for (int e = 0; e < 4; ++e) p[e] = __builtin_elementwise_fma(o2, wf[k][e], p[e]);
    }
    if ((lane & 15) < 10) {
      float* rd = &lds[L_RED + (wave * 4 + r4) * 80 + (lane & 15) * 8];
      *(f32x4*)rd = f32x4{p[0].x, p[0].y, p[1].x, p[1].y};
      *(f32x4*)(rd + 4) = f32x4{p[2].x, p[2].y, p[3].x, p[3].y};
    }
  }
  wg_barrier();
  if (wave == 0) {
    const u32 base2 = WS_HOP2 + (u32)(c * NH) * HOP2_STRIDE;
    if (lane < 40) {
      f32x2 sa = {0.f, 0.f}, sb = {0.f, 0.f}, sc2 = {0.f, 0.f}, sd = {0.f, 0.f};
#pragma unroll
      for (int w = 0; w < 32; w += 4) {
        sa += *(const f32x2*)&lds[L_RED + w * 80 + 2 * lane];
        sb += *(const f32x2*)&lds[L_RED + (w + 1) * 80 + 2 * lane];
        sc2 += *(const f32x2*)&lds[L_RED + (w + 2) * 80 + 2 * lane];
        sd += *(const f32x2*)&lds[L_RED + (w + 3) * 80 + 2 * lane];
      }
      const f32x2 st = (sa + sb) + (sc2 + sd);
      const float s0 = st.x, s1 = st.y;
      u32x4 g;
      g.x = __float_as_uint(s0); g.y = __float_as_uint(s1); g.z = tag_lo; g.w = tag_hi;
      __builtin_amdgcn_raw_buffer_store_b128(g, rw, base2 + h * HOP2_STRIDE + lane * 16, 0, SC1);
    }
    if (reducer) {
      const int pr = lane >> 5;
      u32x4 gg;
      for (u32 spins = 0; spins < (1u << 16); ++spins) {
        const u32 z = opaque0();
        gg = __builtin_amdgcn_raw_buffer_load_b128(rw, base2 + (lane & 31) * HOP2_STRIDE + (2 * h + pr) * 16, z, SC1);
        if (__all(gg.z == tag_lo && gg.w == tag_hi)) break;
        __builtin_amdgcn_s_sleep(1);
      }
      float t0 = __uint_as_float(gg.x), t1 = __uint_as_float(gg.y);
      t0 = ar8_sum(t0); t0 += dpp<0x140>(t0); t0 = swap16_sum(t0);
      t1 = ar8_sum(t1); t1 += dpp<0x140>(t1); t1 = swap16_sum(t1);
      if ((lane & 31) == 0) {
        const float so0 = bf16r(t0), so1 = bf16r(t1);
        const float r0 = bf16r(lo16(xw) + bf16r(so0 * g0)), r1 = bf16r(hi16(xw) + bf16r(so1 * g1));
        out[(c * 80 + 4 * h) / 2 + pr] = (__float_as_uint(r0) >> 16) | (__float_as_uint(r1) & 0xffff0000u);
      }
      if (bid == 0 && lane == 0) {
        u32x2 e; e.x = tag_lo; e.y = tag_hi;
        __builtin_amdgcn_raw_buffer_store_b64(e, rw, WS_EPOCH, 0, SC1);
      }
    }
  }
}

extern "C" void kernel_launch(void* const* d_in, const int* in_sizes, int n_in,
                              void* d_out, int out_size, void* d_ws, size_t ws_size,
                              hipStream_t stream) {
  (void)in_sizes; (void)n_in; (void)out_size; (void)ws_size;
  fused_decode_kernel<<<dim3(NH * NSPLIT), dim3(512), 0, stream>>>(
      (const u32*)d_in[0], (const u32*)d_in[1], (const u32*)d_in[2], (const u32*)d_in[3], (const u32*)d_in[4],
      (const u32*)d_in[6], (const u32*)d_in[7], (const u32*)d_in[8], (const unsigned short*)d_in[9],
      (u32*)d_out, (unsigned char*)d_ws);
}
```
